```python
import torch

def _softmax(x, axis=-1):
    return torch.softmax(x, dim=axis)
def _gelu(x):
    return torch.nn.functional.gelu(x, approximate="tanh")
def _silu(x):
    return torch.nn.functional.silu(x)
def _sigmoid(x):
    return torch.sigmoid(x)
def _layernorm(x, axis=-1, eps=1e-5):
    m = x.mean(dim=axis, keepdim=True)
    v = x.var(dim=axis, keepdim=True, unbiased=False)
    return (x - m) / torch.sqrt(v + eps)
def _scatter_add(tgt, idx, src, axis=0):
    return tgt.clone().index_add_(axis, idx, src)
def _segment_sum(src, seg, n):
    out = torch.zeros((n,) + src.shape[1:], dtype=src.dtype)
    return out.index_add_(0, seg, src)
def _pad(x, pw):
    flat = tuple(v for pair in reversed(pw) for v in pair)
    return torch.nn.functional.pad(x, flat)

kernel_name = 'gated_mix__sliding_block_cached__gqa_block_cached__decode'

def setup_inputs():

    _w = (lambda a: (lambda t: t.to(torch.bfloat16) if t.is_floating_point() else t)(torch.as_tensor(a)))
    def _A_setup():
        B, Sq, D = 1, 1, 448
        Hq, Hkv, Sk, Dh = 16, 8, 8192, 256
        assert Hq % Hkv == 0, (Hq, Hkv)
        return {
            "x": _w((lambda *s: torch.randn(*s, dtype=torch.bfloat16) * 0.05)(B, Sq, D)),
            "k_cache": _w((lambda *s: torch.randn(*s, dtype=torch.bfloat16) * 3.2)(B, Hkv, Sk, Dh)),
            "v_cache": _w((lambda *s: torch.randn(*s, dtype=torch.bfloat16) * 0.05)(B, Hkv, Sk, Dh)),
            "wq": _w((lambda *s: torch.randn(*s, dtype=torch.bfloat16) * 3.2)(D, Hq * Dh) * (D ** -0.5)),
            "wo": _w((lambda *s: torch.randn(*s, dtype=torch.bfloat16) * 0.05)(Hq * Dh, D) * ((Hq * Dh) ** -0.5)),
        }
    _A_in = _A_setup()

    def _B_setup():
        B, Sq, D = 1, 1, 448
        Hq, Hkv, Sk, Dh = 16, 8, 8192, 256
        assert Hq % Hkv == 0, (Hq, Hkv)
        return {
            "x": _w((lambda *s: torch.randn(*s, dtype=torch.bfloat16) * 0.05)(B, Sq, D)),
            "k_cache": _w((lambda *s: torch.randn(*s, dtype=torch.bfloat16) * 3.2)(B, Hkv, Sk, Dh)),
            "v_cache": _w((lambda *s: torch.randn(*s, dtype=torch.bfloat16) * 0.05)(B, Hkv, Sk, Dh)),
            "wq": _w((lambda *s: torch.randn(*s, dtype=torch.bfloat16) * 3.2)(D, Hq * Dh) * (D ** -0.5)),
            "wo": _w((lambda *s: torch.randn(*s, dtype=torch.bfloat16) * 0.05)(Hq * Dh, D) * ((Hq * Dh) ** -0.5)),
        }
    _B_in = _B_setup()

    return {
        "A_x": _A_in["x"],
        "A_k_cache": _A_in["k_cache"],
        "A_v_cache": _A_in["v_cache"],
        "A_wq": _A_in["wq"],
        "A_wo": _A_in["wo"],
        "B_x": _B_in["x"],
        "B_k_cache": _B_in["k_cache"],
        "B_v_cache": _B_in["v_cache"],
        "B_wq": _B_in["wq"],
        "B_wo": _B_in["wo"],
        "gate_w": _w((lambda *s: torch.randn(*s, dtype=torch.bfloat16) * 0.1)(448) * 16.0),
    }

def reference(A_x, A_k_cache, A_v_cache, A_wq, A_wo, B_x, B_k_cache, B_v_cache, B_wq, B_wo, gate_w):
    def _A_ref(x, k_cache, v_cache, wq, wo):
        B, Sq, D = x.shape
        Hq, Dh = 16, 256
        Hkv, Sk = k_cache.shape[1], k_cache.shape[2]
        W = min(4096, max(256, Sk // 4))
        rep = Hq // Hkv
        q = (lambda x, *d: x.permute(*d))((x @ wq).reshape(B, Sq, Hq, Dh), 0, 2, 1, 3)
        k = (lambda x, n, axis: torch.repeat_interleave(x, n, dim=axis))(k_cache[:, :, Sk - W:, :], rep, axis=1)
        v = (lambda x, n, axis: torch.repeat_interleave(x, n, dim=axis))(v_cache[:, :, Sk - W:, :], rep, axis=1)
        scores = torch.einsum("bhqd,bhkd->bhqk", q, k) * (Dh ** -0.5)
        p = _softmax(scores, axis=-1)
        o = torch.einsum("bhqk,bhkd->bhqd", p, v)
        return (lambda x, *d: x.permute(*d))(o, 0, 2, 1, 3).reshape(B, Sq, Hq * Dh) @ wo

    def _B_ref(x, k_cache, v_cache, wq, wo):
        B, Sq, D = x.shape
        Hq, Dh = 16, 256
        Hkv = k_cache.shape[1]
        rep = Hq // Hkv
        q = (lambda x, *d: x.permute(*d))((x @ wq).reshape(B, Sq, Hq, Dh), 0, 2, 1, 3)
        k = (lambda x, n, axis: torch.repeat_interleave(x, n, dim=axis))(k_cache, rep, axis=1)
        v = (lambda x, n, axis: torch.repeat_interleave(x, n, dim=axis))(v_cache, rep, axis=1)
        scores = torch.einsum("bhqd,bhkd->bhqk", q, k) * (Dh ** -0.5)
        p = _softmax(scores, axis=-1)
        o = torch.einsum("bhqk,bhkd->bhqd", p, v)
        return (lambda x, *d: x.permute(*d))(o, 0, 2, 1, 3).reshape(B, Sq, Hq * Dh) @ wo

    x = A_x
    g = _sigmoid((x * gate_w).sum(-1, keepdims=True))
    a = _A_ref(x, A_k_cache, A_v_cache, A_wq, A_wo)
    b = _B_ref(x, B_k_cache, B_v_cache, B_wq, B_wo)
    return g * a + (1 - g) * b
```

```cpp
#include <hip/hip_runtime.h>
#include <hip/hip_bf16.h>
#include <stdint.h>

namespace {

typedef unsigned int u32;
typedef unsigned long long u64;
typedef unsigned char u8;
typedef u32 u32x4 __attribute__((ext_vector_type(4)));
typedef __bf16 bf16x2_t __attribute__((ext_vector_type(2)));

constexpr int kSk = 8192, kWin = 2048;
constexpr int kNWG = 256, kThreads = 512;
constexpr u32 kSpinCap = 1u << 16;
constexpr int kNRed = 56;

constexpr size_t WS_EPOCH = 0;
constexpr size_t WS_QG = 4096;
constexpr size_t WS_PML = 65536;
constexpr size_t WS_Y = 131072;
constexpr size_t WS_PART = 1048576;
constexpr size_t WS_END = WS_PART + (size_t)8 * 32 * 4 * 128 * 16;

#define RLX_AGENT __ATOMIC_RELAXED, __HIP_MEMORY_SCOPE_AGENT

#define STAMP(idx)
#define STAMPV(idx, v)
#define STAMPW(w, idx)

__device__ __forceinline__ float bf_lo(u32 v) { return __uint_as_float(v << 16); }
__device__ __forceinline__ float bf_hi(u32 v) { return __uint_as_float(v & 0xffff0000u); }
__device__ __forceinline__ u32 bf_bits(float f) { return (u32)__bfloat16_as_ushort(__float2bfloat16(f)); }
__device__ __forceinline__ float bf_round(float f) { return __uint_as_float(bf_bits(f) << 16); }

__device__ __forceinline__ u32x4 ld_nt16(const u8* p) {
  return __builtin_nontemporal_load(reinterpret_cast<const u32x4*>(p));
}
__device__ __forceinline__ u64 ld_agent(const u8* p) { return __hip_atomic_load(reinterpret_cast<const u64*>(p), RLX_AGENT); }
__device__ __forceinline__ void st_agent(u8* p, u64 v) { __hip_atomic_store(reinterpret_cast<u64*>(p), v, RLX_AGENT); }
__device__ __forceinline__ void st16_sc1(u8* p, u32x4 v) {
  asm volatile("global_store_dwordx4 %0, %1, off sc1\n\ts_nop 1" ::"v"(p), "v"(v) : "memory");
}
__device__ __forceinline__ u32x4 ld16_sc1(const u8* p) {
  u32x4 a;
  asm volatile("global_load_dwordx4 %0, %1, off sc1\n\ts_waitcnt vmcnt(0)" : "=&v"(a) : "v"(p) : "memory");
  return a;
}
__device__ __forceinline__ void ld16x2_sc1(const u8* p, const u8* q, u32x4& a, u32x4& b) {
  asm volatile(
      "global_load_dwordx4 %0, %2, off sc1\n\t"
      "global_load_dwordx4 %1, %3, off sc1\n\t"
      "s_waitcnt vmcnt(0)"
      : "=&v"(a), "=&v"(b) : "v"(p), "v"(q) : "memory");
}
__device__ __forceinline__ void ld16x4_sc1(const u8* p, const u8* q, u32x4& a, u32x4& b, u32x4& c, u32x4& d) {
  asm volatile(
      "global_load_dwordx4 %0, %4, off sc1\n\t"
      "global_load_dwordx4 %1, %4, off offset:16 sc1\n\t"
      "global_load_dwordx4 %2, %5, off sc1\n\t"
      "global_load_dwordx4 %3, %5, off offset:16 sc1\n\t"
      "s_waitcnt vmcnt(0)"
      : "=&v"(a), "=&v"(b), "=&v"(c), "=&v"(d) : "v"(p), "v"(q) : "memory");
}
__device__ __forceinline__ u64 rec_hash(u32 a, u32 b) {
  const u64 t = (((u64)b << 32) | (u64)a) * 0x9E3779B97F4A7C15ull;
  return t ^ (t >> 32);
}
__device__ __forceinline__ void publish2(u8* p, u32 a, u32 b, u64 F) {
  const u64 c = F ^ rec_hash(a, b);
  st16_sc1(p, u32x4{a, b, (u32)c, (u32)(c >> 32)});
}
__device__ __forceinline__ bool rec_ok(const u32x4& r, u64 F) {
  return ((((u64)r.w << 32) | (u64)r.z) ^ rec_hash(r.x, r.y)) == F;
}

__device__ __forceinline__ void wg_barrier() {
  asm volatile("s_waitcnt lgkmcnt(0)" ::: "memory");
  __builtin_amdgcn_s_barrier();
  asm volatile("" ::: "memory");
}

template <int K>
__device__ __forceinline__ float swz_xor(float x) {
  return __uint_as_float((u32)__builtin_amdgcn_ds_swizzle((int)__float_as_uint(x), (K << 10) | 0x1F));
}
template <int K>
__device__ __forceinline__ u32 swz_xor_u(u32 x) { return (u32)__builtin_amdgcn_ds_swizzle((int)x, (K << 10) | 0x1F); }
__device__ __forceinline__ float xor32(float x) { return __shfl_xor(x, 32, 64); }

__device__ __forceinline__ float dot2(u32 a, u32 b, float c) {
  return __builtin_amdgcn_fdot2_f32_bf16(__builtin_bit_cast(bf16x2_t, a), __builtin_bit_cast(bf16x2_t, b), c, false);
}
__device__ __forceinline__ float dot8(const u32x4& k, const u32x4& q) {
  float s = dot2(k.x, q.x, 0.f);
  s = dot2(k.y, q.y, s);
  s = dot2(k.z, q.z, s);
  s = dot2(k.w, q.w, s);
  return s;
}

template <int BIT, int N>
__device__ __forceinline__ void tr_step(float* x, int c) {
  const bool sel = (c & BIT) != 0;
#pragma unroll
  for (int i = 0; i < N / 2; ++i) {
    const float lo = x[i], hi = x[i + N / 2];
    const float send = sel ? lo : hi;
    const float keep = sel ? hi : lo;
    x[i] = keep + swz_xor<BIT>(send);
  }
}

__device__ __forceinline__ void pv_slot(const u32x4& V, float p0, float p1, float* a0, float* a1) {
  float v[8] = {bf_lo(V.x), bf_hi(V.x), bf_lo(V.y), bf_hi(V.y), bf_lo(V.z), bf_hi(V.z), bf_lo(V.w), bf_hi(V.w)};
#pragma unroll
  for (int k = 0; k < 8; ++k) {
    a0[k] = fmaf(p0, v[k], a0[k]);
    a1[k] = fmaf(p1, v[k], a1[k]);
  }
}

__device__ __forceinline__ void fma8(float* acc, float xi, const u32x4& w) {
  acc[0] = fmaf(xi, bf_lo(w.x), acc[0]);
  acc[1] = fmaf(xi, bf_hi(w.x), acc[1]);
  acc[2] = fmaf(xi, bf_lo(w.y), acc[2]);
  acc[3] = fmaf(xi, bf_hi(w.y), acc[3]);
  acc[4] = fmaf(xi, bf_lo(w.z), acc[4]);
  acc[5] = fmaf(xi, bf_hi(w.z), acc[5]);
  acc[6] = fmaf(xi, bf_lo(w.w), acc[6]);
  acc[7] = fmaf(xi, bf_hi(w.w), acc[7]);
}

__global__ __launch_bounds__(kThreads, 2) void gm_kernel(
    const u8* __restrict__ xin, const u8* __restrict__ kA, const u8* __restrict__ vA,
    const u8* __restrict__ wqA, const u8* __restrict__ woA, const u8* __restrict__ kB,
    const u8* __restrict__ vB, const u8* __restrict__ wqB, const u8* __restrict__ woB,
    const u8* __restrict__ gatew, unsigned short* __restrict__ out, u8* __restrict__ ws) {
  __shared__ __attribute__((aligned(16))) float s_red[8][4][256];
  __shared__ __attribute__((aligned(16))) u32 s_q[2][256];
  __shared__ __attribute__((aligned(16))) float s_p[8][2][64];
  __shared__ __attribute__((aligned(16))) float s_ml[8][4][2];
  __shared__ __attribute__((aligned(16))) float s_o[8][32];
  __shared__ __attribute__((aligned(16))) u32 s_flag[4];

  const int tid = threadIdx.x;
  const int lane = tid & 63;
  const int wave = __builtin_amdgcn_readfirstlane(tid >> 6);
  const int wg = blockIdx.x;
  const int h = wg & 7, j = wg >> 3;
  const int hf = lane >> 5, c = lane & 31;
  STAMPW(0, 0);

  const int c4 = j >> 3;
  const int ob = c4 >> 1, oqh = c4 & 1;
  const int chunk = j & 7;
  const int cl = lane < 56 ? lane : 55;

  const int bq = j >> 4;
  const int col0 = 512 * h + 32 * (j & 15);
  const int rr = lane >> 2, cc = lane & 3;
  const int rrc = rr < 14 ? rr : 13;
  const size_t offA = ((size_t)h * kSk + (kSk - kWin) + 64 * j + 8 * wave) * 512 + lane * 16;
  const size_t offB = ((size_t)h * kSk + 256 * j + 32 * wave) * 512 + lane * 16;
  u32x4 KA[4], KB[16], VA[4], VB[16], WO[4], GX, GW;
  u64 E = 0;
  if (wave == 0) E = ld_agent(ws + WS_EPOCH);
  {
    const u8* wq = bq ? wqB : wqA;
    const u8* wp = wq + (size_t)(56 * wave + 4 * rrc) * 8192 + (size_t)(col0 + 8 * cc) * 2;
    u32x4 wv[4];
#pragma unroll
    for (int i = 0; i < 4; ++i) wv[i] = ld_nt16(wp + (size_t)i * 8192);
    uint2 xv = *reinterpret_cast<const uint2*>(xin + 2 * (56 * wave + 4 * rrc));
    asm volatile("" ::: "memory");
    __builtin_amdgcn_s_barrier();
    asm volatile("" ::: "memory");
#pragma unroll
    for (int i = 0; i < 4; ++i) KA[i] = ld_nt16(kA + offA + i * 1024);
#pragma unroll
    for (int i = 0; i < 4; ++i) KB[i] = ld_nt16(kB + offB + i * 1024);
    asm volatile("" ::: "memory");
    if (rr >= 14) xv = make_uint2(0u, 0u);
    if (wave == 0 && lane == 0) s_flag[0] = 0u;

    float acc[8];
#pragma unroll
    for (int k = 0; k < 8; ++k) acc[k] = 0.f;
    fma8(acc, bf_lo(xv.x), wv[0]);
    fma8(acc, bf_hi(xv.x), wv[1]);
    fma8(acc, bf_lo(xv.y), wv[2]);
    fma8(acc, bf_hi(xv.y), wv[3]);
    tr_step<4, 8>(acc, lane);
    tr_step<8, 4>(acc, lane);
    acc[0] += swz_xor<16>(acc[0]);
    acc[1] += swz_xor<16>(acc[1]);
    acc[0] += xor32(acc[0]);
    acc[1] += xor32(acc[1]);
    if (lane < 16) *reinterpret_cast<float2*>(&s_o[wave][2 * lane]) = make_float2(acc[0], acc[1]);
  }

  wg_barrier();
  STAMPW(0, 1);

  if (wave == 0) {
    const u64 F = E + 1;
    float s0 = 0.f, s1 = 0.f;
#pragma unroll
    for (int w = 0; w < 8; ++w) {
      const float2 t = *reinterpret_cast<const float2*>(&s_o[w][2 * (lane & 15)]);
      s0 += t.x;
      s1 += t.y;
    }
    {
      const u32 pr = bf_bits(s0) | (bf_bits(s1) << 16);
      const u32 other = swz_xor_u<8>(pr);
      if (lane < 16 && (lane & 8) == 0)
        publish2(ws + WS_QG + (size_t)(bq * 1024 + (col0 >> 2) + 2 * cc + ((lane >> 2) & 1)) * 16, pr, other, F);
    }
    STAMP(3);
    const u8* qa = ws + WS_QG + (size_t)(128 * h + 2 * lane) * 16;
    const u8* qb = qa + (size_t)1024 * 16;
    u32x4 r0 = {0u, 0u, 0u, 0u}, r1 = r0, r2 = r0, r3 = r0;
    bool ok = false;
    for (u32 spin = 0;; ++spin) {
      if (!ok) {
        ld16x4_sc1(qa, qb, r0, r1, r2, r3);
        ok = rec_ok(r0, F) && rec_ok(r1, F) && rec_ok(r2, F) && rec_ok(r3, F);
      }
      if (spin == 0) STAMP(20);
      STAMPV(21, spin + 1);
      if (__all(ok)) break;
      if (spin > kSpinCap) break;
      __builtin_amdgcn_s_sleep(1);
    }
    *reinterpret_cast<u32x4*>(&s_q[0][4 * lane]) = u32x4{r0.x, r0.y, r1.x, r1.y};
    *reinterpret_cast<u32x4*>(&s_q[1][4 * lane]) = u32x4{r2.x, r2.y, r3.x, r3.y};
    if (lane == 0) { s_flag[2] = (u32)E; s_flag[3] = (u32)(E >> 32); }
    asm volatile("s_waitcnt lgkmcnt(0)" ::: "memory");
    if (lane == 0) __hip_atomic_store(&s_flag[0], 1u, __ATOMIC_RELAXED, __HIP_MEMORY_SCOPE_WORKGROUP);
    asm volatile("s_waitcnt lgkmcnt(0)" ::: "memory");
    STAMP(4);
  }

#pragma unroll
  for (int i = 4; i < 16; ++i) KB[i] = ld_nt16(kB + offB + i * 1024);
#pragma unroll
  for (int i = 0; i < 4; ++i) VA[i] = ld_nt16(vA + offA + i * 1024);
#pragma unroll
  for (int i = 0; i < 6; ++i) VB[i] = ld_nt16(vB + offB + i * 1024);
  STAMPW(1, 2);

  {
    u32 spin = 0;
    while (__builtin_amdgcn_readfirstlane(__hip_atomic_load(&s_flag[0], __ATOMIC_RELAXED, __HIP_MEMORY_SCOPE_WORKGROUP)) == 0u) {
      if (++spin > (kSpinCap << 4)) break;
      __builtin_amdgcn_s_sleep(2);
    }
    asm volatile("" ::: "memory");
  }
  STAMPW(1, 5);
  STAMPW(0, 12);
  u64 F;
  {
    const u32 elo = __builtin_amdgcn_readfirstlane(s_flag[2]);
    const u32 ehi = __builtin_amdgcn_readfirstlane(s_flag[3]);
    F = (((u64)ehi << 32) | (u64)elo) + 1;
  }

  const u32x4 qa0 = *reinterpret_cast<const u32x4*>(&s_q[0][4 * c]);
  const u32x4 qa1 = *reinterpret_cast<const u32x4*>(&s_q[0][128 + 4 * c]);
  const u32x4 qb0 = *reinterpret_cast<const u32x4*>(&s_q[1][4 * c]);
  const u32x4 qb1 = *reinterpret_cast<const u32x4*>(&s_q[1][128 + 4 * c]);

  constexpr float kSc = 0.0625f * 1.4426950408889634f;
  float mA, lA;
  {
    float xa[8];
#pragma unroll
    for (int i = 0; i < 4; ++i) {
      xa[2 * i] = dot8(KA[i], qa0);
      xa[2 * i + 1] = dot8(KA[i], qa1);
    }
    tr_step<4, 8>(xa, c);
    tr_step<2, 4>(xa, c);
    tr_step<1, 2>(xa, c);
    float s = xa[0];
    s += swz_xor<8>(s);
    s += swz_xor<16>(s);
    s = bf_round(s) * kSc;
    float m = s;
    m = fmaxf(m, swz_xor<2>(m));
    m = fmaxf(m, swz_xor<4>(m));
    m = fmaxf(m, xor32(m));
    const float p = __builtin_amdgcn_exp2f(s - m);
    float l = p;
    l += swz_xor<2>(l);
    l += swz_xor<4>(l);
    l += xor32(l);
    mA = m;
    lA = l;
    s_p[wave][0][lane] = p;
  }
  float mB, lB;
  {
    float xb[32];
#pragma unroll
    for (int i = 0; i < 16; ++i) {
      xb[2 * i] = dot8(KB[i], qb0);
      xb[2 * i + 1] = dot8(KB[i], qb1);
    }
    tr_step<16, 32>(xb, c);
    tr_step<8, 16>(xb, c);
    tr_step<4, 8>(xb, c);
    tr_step<2, 4>(xb, c);
    tr_step<1, 2>(xb, c);
    const float s = bf_round(xb[0]) * kSc;
    float m = s;
    m = fmaxf(m, swz_xor<2>(m));
    m = fmaxf(m, swz_xor<4>(m));
    m = fmaxf(m, swz_xor<8>(m));
    m = fmaxf(m, swz_xor<16>(m));
    m = fmaxf(m, xor32(m));
    const float p = __builtin_amdgcn_exp2f(s - m);
    float l = p;
    l += swz_xor<2>(l);
    l += swz_xor<4>(l);
    l += swz_xor<8>(l);
    l += swz_xor<16>(l);
    l += xor32(l);
    mB = m;
    lB = l;
    s_p[wave][1][lane] = p;
  }
  if (lane < 2) {
    s_ml[wave][lane][0] = mA;
    s_ml[wave][lane][1] = lA;
    s_ml[wave][2 + lane][0] = mB;
    s_ml[wave][2 + lane][1] = lB;
  }
  asm volatile("s_waitcnt lgkmcnt(0)" ::: "memory");
  STAMPW(1, 6);

#pragma unroll
  for (int i = 6; i < 16; ++i) VB[i] = ld_nt16(vB + offB + i * 1024);
  {
    const u8* wo = ob ? woB : woA;
    const size_t row0 = (size_t)(2 * h + oqh) * 256 + 32 * chunk + 4 * wave;
#pragma unroll
    for (int r = 0; r < 4; ++r) WO[r] = ld_nt16(wo + (row0 + r) * 896 + cl * 16);
  }
  GX = *reinterpret_cast<const u32x4*>(xin + cl * 16);
  GW = *reinterpret_cast<const u32x4*>(gatew + cl * 16);
  asm volatile("" ::: "memory");

  float a0[8], a1[8], b0[8], b1[8];
#pragma unroll
  for (int k = 0; k < 8; ++k) a0[k] = a1[k] = b0[k] = b1[k] = 0.f;
#pragma unroll
  for (int i = 0; i < 4; ++i) {
    const float2 pp = *reinterpret_cast<const float2*>(&s_p[wave][0][hf * 32 + 2 * i]);
    pv_slot(VA[i], pp.x, pp.y, a0, a1);
  }
#pragma unroll
  for (int i = 0; i < 16; ++i) {
    const float2 pp = *reinterpret_cast<const float2*>(&s_p[wave][1][hf * 32 + 2 * i]);
    pv_slot(VB[i], pp.x, pp.y, b0, b1);
  }
  STAMPW(1, 7);
  STAMPW(0, 13);
  STAMP(22 + wave);
  {
    float r0[8], r1[8];
#pragma unroll
    for (int k = 0; k < 8; ++k) {
      const float sa0 = hf ? a0[k] : b0[k], sa1 = hf ? a1[k] : b1[k];
      const float ka0 = hf ? b0[k] : a0[k], ka1 = hf ? b1[k] : a1[k];
      r0[k] = ka0 + xor32(sa0);
      r1[k] = ka1 + xor32(sa1);
    }
    float4* d0 = reinterpret_cast<float4*>(&s_red[wave][2 * hf][8 * c]);
    float4* d1 = reinterpret_cast<float4*>(&s_red[wave][2 * hf + 1][8 * c]);
    d0[0] = make_float4(r0[0], r0[1], r0[2], r0[3]);
    d0[1] = make_float4(r0[4], r0[5], r0[6], r0[7]);
    d1[0] = make_float4(r1[0], r1[1], r1[2], r1[3]);
    d1[1] = make_float4(r1[4], r1[5], r1[6], r1[7]);
  }
  wg_barrier();
  STAMPW(1, 8);
  {
    const int combo = tid >> 7, dp = tid & 127;
    float M = s_ml[0][combo][0];
#pragma unroll
    for (int w = 1; w < 8; ++w) M = fmaxf(M, s_ml[w][combo][0]);
    float L = 0.f, o0 = 0.f, o1 = 0.f;
#pragma unroll
    for (int w = 0; w < 8; ++w) {
      const float sc = __builtin_amdgcn_exp2f(s_ml[w][combo][0] - M);
      const float2 t = *reinterpret_cast<const float2*>(&s_red[w][combo][2 * dp]);
      L = fmaf(sc, s_ml[w][combo][1], L);
      o0 = fmaf(sc, t.x, o0);
      o1 = fmaf(sc, t.y, o1);
    }
    publish2(ws + WS_PART + ((size_t)(h * 32 + j) * 512 + tid) * 16, __float_as_uint(o0), __float_as_uint(o1), F);
    if (dp == 0) publish2(ws + WS_PML + ((size_t)(h * 32 + j) * 4 + combo) * 16, __float_as_uint(M), __float_as_uint(L), F);
    STAMPW(1, 9);
  }

  {
    const int sl = tid >> 4, r = tid & 15;
    const u8* pv = ws + WS_PART + ((size_t)(h * 32 + sl) * 512 + c4 * 128 + chunk * 16 + r) * 16;
    const u8* pm = ws + WS_PML + ((size_t)(h * 32 + c) * 4 + c4) * 16;
    u32x4 rv = {0u, 0u, 0u, 0u}, rm = rv;
    bool ok = false;
    if (wave == 0) {
      for (u32 spin = 0;; ++spin) {
        if (!ok) {
          rm = ld16_sc1(pm);
          ok = rec_ok(rm, F);
        }
        if (__all(ok)) break;
        if (spin > kSpinCap) break;
        __builtin_amdgcn_s_sleep(1);
      }
      ok = false;
    }
    wg_barrier();
    for (u32 spin = 0;; ++spin) {
      if (!ok) {
        ld16x2_sc1(pv, pm, rv, rm);
        ok = rec_ok(rv, F) && rec_ok(rm, F);
      }
      if (spin == 0) STAMPW(1, 16);
      if (wave == 1) STAMPV(17, spin + 1);
      if (__all(ok)) break;
      if (spin > kSpinCap) break;
      __builtin_amdgcn_s_sleep(1);
    }
    STAMPW(1, 10);
    const float ms = __uint_as_float(rm.x), ls = __uint_as_float(rm.y);
    float M = ms;
    M = fmaxf(M, swz_xor<1>(M));
    M = fmaxf(M, swz_xor<2>(M));
    M = fmaxf(M, swz_xor<4>(M));
    M = fmaxf(M, swz_xor<8>(M));
    M = fmaxf(M, swz_xor<16>(M));
    const float wsc = __builtin_amdgcn_exp2f(ms - M);
    float L = wsc * ls;
    L += swz_xor<1>(L);
    L += swz_xor<2>(L);
    L += swz_xor<4>(L);
    L += swz_xor<8>(L);
    L += swz_xor<16>(L);
    const float wmine = __shfl(wsc, sl & 31, 64) * (1.0f / L);
    float o0 = wmine * __uint_as_float(rv.x), o1 = wmine * __uint_as_float(rv.y);
    o0 += swz_xor<16>(o0);
    o1 += swz_xor<16>(o1);
    o0 += xor32(o0);
    o1 += xor32(o1);
    if (lane < 16) *reinterpret_cast<float2*>(&s_o[wave][2 * lane]) = make_float2(o0, o1);
    wg_barrier();
    float od[4] = {0.f, 0.f, 0.f, 0.f};
#pragma unroll
    for (int w = 0; w < 8; ++w) {
      const float4 t = *reinterpret_cast<const float4*>(&s_o[w][4 * wave]);
      od[0] += t.x;
      od[1] += t.y;
      od[2] += t.z;
      od[3] += t.w;
    }
    float y[8];
#pragma unroll
    for (int k = 0; k < 8; ++k) y[k] = 0.f;
#pragma unroll
    for (int q = 0; q < 4; ++q) fma8(y, bf_round(od[q]), WO[q]);
    float* yred = &s_red[0][0][0];
    if (lane < 56) {
      float4* d = reinterpret_cast<float4*>(yred + wave * 448 + 8 * lane);
      d[0] = make_float4(y[0], y[1], y[2], y[3]);
      d[1] = make_float4(y[4], y[5], y[6], y[7]);
    }
    wg_barrier();
    if (tid < 224) {
      float s0 = 0.f, s1 = 0.f;
#pragma unroll
      for (int w = 0; w < 8; ++w) {
        const float2 t = *reinterpret_cast<const float2*>(yred + w * 448 + 2 * tid);
        s0 += t.x;
        s1 += t.y;
      }
      publish2(ws + WS_Y + ((size_t)wg * 224 + tid) * 16, __float_as_uint(s0), __float_as_uint(s1), F);
      STAMPW(1, 11);
    }
  }

  if (wg < kNRed) {
    const int p = tid >> 1, hh = tid & 1;
    const u8* yp = ws + WS_Y + ((size_t)p * 224 + 4 * wg + 2 * hh) * 16;
    u32x4 r0 = {0u, 0u, 0u, 0u}, r1 = r0;
    bool ok = false;
    for (u32 spin = 0;; ++spin) {
      if (!ok) {
        ld16x2_sc1(yp, yp + 16, r0, r1);
        ok = rec_ok(r0, F) && rec_ok(r1, F);
      }
      if (spin == 0) STAMPW(0, 18);
      if (wave == 0) STAMPV(19, spin + 1);
      if (__all(ok)) break;
      if (spin > kSpinCap) break;
      __builtin_amdgcn_s_sleep(1);
    }
    STAMPW(0, 14);
    float v0 = __uint_as_float(r0.x), v1 = __uint_as_float(r0.y), v2 = __uint_as_float(r1.x), v3 = __uint_as_float(r1.y);
    v0 += swz_xor<2>(v0); v1 += swz_xor<2>(v1); v2 += swz_xor<2>(v2); v3 += swz_xor<2>(v3);
    v0 += swz_xor<4>(v0); v1 += swz_xor<4>(v1); v2 += swz_xor<4>(v2); v3 += swz_xor<4>(v3);
    v0 += swz_xor<8>(v0); v1 += swz_xor<8>(v1); v2 += swz_xor<8>(v2); v3 += swz_xor<8>(v3);
    v0 += swz_xor<16>(v0); v1 += swz_xor<16>(v1); v2 += swz_xor<16>(v2); v3 += swz_xor<16>(v3);
    v0 += xor32(v0); v1 += xor32(v1); v2 += xor32(v2); v3 += xor32(v3);
    if (lane < 2) *reinterpret_cast<float4*>(&s_o[wave][4 * lane]) = make_float4(v0, v1, v2, v3);
    wg_barrier();
    if (wave == 0) {
      float gs = 0.f;
      if (lane < 56) {
        gs += bf_round(bf_lo(GX.x) * bf_lo(GW.x));
        gs += bf_round(bf_hi(GX.x) * bf_hi(GW.x));
        gs += bf_round(bf_lo(GX.y) * bf_lo(GW.y));
        gs += bf_round(bf_hi(GX.y) * bf_hi(GW.y));
        gs += bf_round(bf_lo(GX.z) * bf_lo(GW.z));
        gs += bf_round(bf_hi(GX.z) * bf_hi(GW.z));
        gs += bf_round(bf_lo(GX.w) * bf_lo(GW.w));
        gs += bf_round(bf_hi(GX.w) * bf_hi(GW.w));
      }
      gs += swz_xor<1>(gs);
      gs += swz_xor<2>(gs);
      gs += swz_xor<4>(gs);
      gs += swz_xor<8>(gs);
      gs += swz_xor<16>(gs);
      gs += xor32(gs);
      gs = bf_round(gs);
      const float g = bf_round(1.0f / (1.0f + __expf(-gs)));
      const float omg = bf_round(1.0f - g);
      if (lane < 4) {
        float ra[2], rb[2];
#pragma unroll
        for (int e = 0; e < 2; ++e) {
          const int col = 2 * lane + e;
          ra[e] = (s_o[0][col] + s_o[1][col]) + (s_o[2][col] + s_o[3][col]);
          rb[e] = (s_o[4][col] + s_o[5][col]) + (s_o[6][col] + s_o[7][col]);
        }
        const float r0f = bf_round(g * bf_round(ra[0])) + bf_round(omg * bf_round(rb[0]));
        const float r1f = bf_round(g * bf_round(ra[1])) + bf_round(omg * bf_round(rb[1]));
        *reinterpret_cast<u32*>(out + 8 * wg + 2 * lane) = bf_bits(r0f) | (bf_bits(r1f) << 16);
      }
      if (wg == 0 && lane == 0) st_agent(ws + WS_EPOCH, F);
      STAMP(15);
    }
  }
}

}

extern "C" void kernel_launch(void* const* d_in, const int* in_sizes, int n_in, void* d_out, int out_size,
                              void* d_ws, size_t ws_size, hipStream_t stream) {
  (void)in_sizes; (void)n_in; (void)out_size; (void)ws_size;
  const u8* x = (const u8*)d_in[0];
  const u8* kA = (const u8*)d_in[1];
  const u8* vA = (const u8*)d_in[2];
  const u8* wqA = (const u8*)d_in[3];
  const u8* woA = (const u8*)d_in[4];
  const u8* kB = (const u8*)d_in[6];
  const u8* vB = (const u8*)d_in[7];
  const u8* wqB = (const u8*)d_in[8];
  const u8* woB = (const u8*)d_in[9];
  const u8* gw = (const u8*)d_in[10];
  gm_kernel<<<kNWG, kThreads, 0, stream>>>(x, kA, vA, wqA, woA, kB, vB, wqB, woB, gw, (unsigned short*)d_out, (u8*)d_ws);
}
```

```cpp
#include <hip/hip_runtime.h>
#include <hip/hip_bf16.h>
#include <stdint.h>

namespace {

typedef unsigned int u32;
typedef unsigned long long u64;
typedef unsigned char u8;
typedef u32 u32x4 __attribute__((ext_vector_type(4)));
typedef __bf16 bf16x2_t __attribute__((ext_vector_type(2)));

constexpr int kSk = 8192, kWin = 2048;
constexpr int kNWG = 256, kThreads = 512;
constexpr u32 kSpinCap = 1u << 16;
#define GM_VLATE 10
constexpr int kNRed = 56;

constexpr size_t WS_EPOCH = 0;
constexpr size_t WS_QG = 4096;
constexpr size_t WS_PML = 65536;
constexpr size_t WS_Y = 131072;
constexpr size_t WS_PART = 1048576;
constexpr size_t WS_END = WS_PART + (size_t)8 * 32 * 4 * 128 * 16;

#define RLX_AGENT __ATOMIC_RELAXED, __HIP_MEMORY_SCOPE_AGENT

#define STAMP(idx)
#define STAMPV(idx, v)
#define STAMPW(w, idx)

__device__ __forceinline__ float bf_lo(u32 v) { return __uint_as_float(v << 16); }
__device__ __forceinline__ float bf_hi(u32 v) { return __uint_as_float(v & 0xffff0000u); }
__device__ __forceinline__ u32 bf_bits(float f) { return (u32)__bfloat16_as_ushort(__float2bfloat16(f)); }
__device__ __forceinline__ float bf_round(float f) { return __uint_as_float(bf_bits(f) << 16); }

__device__ __forceinline__ u32x4 ld_nt16(const u8* p) {
  return __builtin_nontemporal_load(reinterpret_cast<const u32x4*>(p));
}
__device__ __forceinline__ u64 ld_agent(const u8* p) { return __hip_atomic_load(reinterpret_cast<const u64*>(p), RLX_AGENT); }
__device__ __forceinline__ void st_agent(u8* p, u64 v) { __hip_atomic_store(reinterpret_cast<u64*>(p), v, RLX_AGENT); }
__device__ __forceinline__ void st16_sc1(u8* p, u32x4 v) {
  asm volatile("global_store_dwordx4 %0, %1, off sc1\n\ts_nop 1" ::"v"(p), "v"(v) : "memory");
}
__device__ __forceinline__ u32x4 ld16_sc1(const u8* p) {
  u32x4 a;
  asm volatile("global_load_dwordx4 %0, %1, off sc1\n\ts_waitcnt vmcnt(0)" : "=&v"(a) : "v"(p) : "memory");
  return a;
}
__device__ __forceinline__ void ld16x2_sc1(const u8* p, const u8* q, u32x4& a, u32x4& b) {
  asm volatile(
      "global_load_dwordx4 %0, %2, off sc1\n\t"
      "global_load_dwordx4 %1, %3, off sc1\n\t"
      "s_waitcnt vmcnt(0)"
      : "=&v"(a), "=&v"(b) : "v"(p), "v"(q) : "memory");
}
__device__ __forceinline__ void ld16x4_sc1(const u8* p, const u8* q, u32x4& a, u32x4& b, u32x4& c, u32x4& d) {
  asm volatile(
      "global_load_dwordx4 %0, %4, off sc1\n\t"
      "global_load_dwordx4 %1, %4, off offset:16 sc1\n\t"
      "global_load_dwordx4 %2, %5, off sc1\n\t"
      "global_load_dwordx4 %3, %5, off offset:16 sc1\n\t"
      "s_waitcnt vmcnt(0)"
      : "=&v"(a), "=&v"(b), "=&v"(c), "=&v"(d) : "v"(p), "v"(q) : "memory");
}
__device__ __forceinline__ u64 rec_hash(u32 a, u32 b) {
  const u64 t = (((u64)b << 32) | (u64)a) * 0x9E3779B97F4A7C15ull;
  return t ^ (t >> 32);
}
__device__ __forceinline__ void publish2(u8* p, u32 a, u32 b, u64 F) {
  const u64 c = F ^ rec_hash(a, b);
  st16_sc1(p, u32x4{a, b, (u32)c, (u32)(c >> 32)});
}
__device__ __forceinline__ bool rec_ok(const u32x4& r, u64 F) {
  return ((((u64)r.w << 32) | (u64)r.z) ^ rec_hash(r.x, r.y)) == F;
}

__device__ __forceinline__ void wg_barrier() {
  asm volatile("s_waitcnt lgkmcnt(0)" ::: "memory");
  __builtin_amdgcn_s_barrier();
  asm volatile("" ::: "memory");
}

template <int K>
__device__ __forceinline__ float swz_xor(float x) {
  return __uint_as_float((u32)__builtin_amdgcn_ds_swizzle((int)__float_as_uint(x), (K << 10) | 0x1F));
}
template <int K>
__device__ __forceinline__ u32 swz_xor_u(u32 x) { return (u32)__builtin_amdgcn_ds_swizzle((int)x, (K << 10) | 0x1F); }
__device__ __forceinline__ float xor32(float x) { return __shfl_xor(x, 32, 64); }

template <int N>
__device__ __forceinline__ float dpp_ror(float x) {
  return __uint_as_float((u32)__builtin_amdgcn_update_dpp(0, (int)__float_as_uint(x), 0x120 + N, 0xf, 0xf, false));
}
__device__ __forceinline__ float x16(float x, bool mx) {
  const auto s = __builtin_amdgcn_permlane16_swap(__float_as_uint(x), __float_as_uint(x), false, false);
  return mx ? fmaxf(__uint_as_float(s[0]), __uint_as_float(s[1])) : __uint_as_float(s[0]) + __uint_as_float(s[1]);
}
__device__ __forceinline__ float x32(float x, bool mx) {
  const auto s = __builtin_amdgcn_permlane32_swap(__float_as_uint(x), __float_as_uint(x), false, false);
  return mx ? fmaxf(__uint_as_float(s[0]), __uint_as_float(s[1])) : __uint_as_float(s[0]) + __uint_as_float(s[1]);
}
__device__ __forceinline__ float px32(float x, int hf) {
  const auto s = __builtin_amdgcn_permlane32_swap(__float_as_uint(x), __float_as_uint(x), false, false);
  return __uint_as_float(hf ? s[0] : s[1]);
}
__device__ __forceinline__ float sum16(float x) {
  x += dpp_ror<8>(x);
  x += dpp_ror<4>(x);
  x += dpp_ror<2>(x);
  x += dpp_ror<1>(x);
  return x;
}
__device__ __forceinline__ float max16(float x) {
  x = fmaxf(x, dpp_ror<8>(x));
  x = fmaxf(x, dpp_ror<4>(x));
  x = fmaxf(x, dpp_ror<2>(x));
  x = fmaxf(x, dpp_ror<1>(x));
  return x;
}

__device__ __forceinline__ float dot2(u32 a, u32 b, float c) {
  return __builtin_amdgcn_fdot2_f32_bf16(__builtin_bit_cast(bf16x2_t, a), __builtin_bit_cast(bf16x2_t, b), c, false);
}
__device__ __forceinline__ float dot8(const u32x4& k, const u32x4& q) {
  float s = dot2(k.x, q.x, 0.f);
  s = dot2(k.y, q.y, s);
  s = dot2(k.z, q.z, s);
  s = dot2(k.w, q.w, s);
  return s;
}

template <int BIT, int N>
__device__ __forceinline__ void tr_step(float* x, int c) {
  const bool sel = (c & BIT) != 0;
#pragma unroll
  for (int i = 0; i < N / 2; ++i) {
    const float lo = x[i], hi = x[i + N / 2];
    const float send = sel ? lo : hi;
    const float keep = sel ? hi : lo;
    x[i] = keep + swz_xor<BIT>(send);
  }
}

__device__ __forceinline__ void pv_slot(const u32x4& V, float p0, float p1, float* a0, float* a1) {
  float v[8] = {bf_lo(V.x), bf_hi(V.x), bf_lo(V.y), bf_hi(V.y), bf_lo(V.z), bf_hi(V.z), bf_lo(V.w), bf_hi(V.w)};
#pragma unroll
  for (int k = 0; k < 8; ++k) {
    a0[k] = fmaf(p0, v[k], a0[k]);
    a1[k] = fmaf(p1, v[k], a1[k]);
  }
}

__device__ __forceinline__ void fma8(float* acc, float xi, const u32x4& w) {
  acc[0] = fmaf(xi, bf_lo(w.x), acc[0]);
  acc[1] = fmaf(xi, bf_hi(w.x), acc[1]);
  acc[2] = fmaf(xi, bf_lo(w.y), acc[2]);
  acc[3] = fmaf(xi, bf_hi(w.y), acc[3]);
  acc[4] = fmaf(xi, bf_lo(w.z), acc[4]);
  acc[5] = fmaf(xi, bf_hi(w.z), acc[5]);
  acc[6] = fmaf(xi, bf_lo(w.w), acc[6]);
  acc[7] = fmaf(xi, bf_hi(w.w), acc[7]);
}

__global__ __launch_bounds__(kThreads, 2) void gm_kernel(
    const u8* __restrict__ xin, const u8* __restrict__ kA, const u8* __restrict__ vA,
    const u8* __restrict__ wqA, const u8* __restrict__ woA, const u8* __restrict__ kB,
    const u8* __restrict__ vB, const u8* __restrict__ wqB, const u8* __restrict__ woB,
    const u8* __restrict__ gatew, unsigned short* __restrict__ out, u8* __restrict__ ws) {
  __shared__ __attribute__((aligned(16))) float s_red[8][4][256];
  __shared__ __attribute__((aligned(16))) u32 s_q[2][256];
  __shared__ __attribute__((aligned(16))) float s_p[8][2][64];
  __shared__ __attribute__((aligned(16))) float s_ml[8][4][2];
  __shared__ __attribute__((aligned(16))) float s_o[8][32];
  __shared__ __attribute__((aligned(16))) float s_w[32];
  __shared__ __attribute__((aligned(16))) u32 s_flag[4];

  const int tid = threadIdx.x;
  const int lane = tid & 63;
  const int wave = __builtin_amdgcn_readfirstlane(tid >> 6);
  const int wg = blockIdx.x;
  const int h = wg & 7, j = wg >> 3;
  const int hf = lane >> 5, c = lane & 31;
  STAMPW(0, 0);

  const int c4 = j >> 3;
  const int ob = c4 >> 1, oqh = c4 & 1;
  const int chunk = j & 7;
  const int cl = lane < 56 ? lane : 55;

  const int bq = j >> 4;
  const int col0 = 512 * h + 32 * (j & 15);
  const int rr = lane >> 2, cc = lane & 3;
  const int rrc = rr < 14 ? rr : 13;
  const size_t offA = ((size_t)h * kSk + (kSk - kWin) + 64 * j + 8 * wave) * 512 + lane * 16;
  const size_t offB = ((size_t)h * kSk + 256 * j + 32 * wave) * 512 + lane * 16;
  u32x4 KA[4], KB[16], VA[4], VB[16], WO[4], GX, GW;
  u64 E = 0;
  if (wave == 0) E = ld_agent(ws + WS_EPOCH);
  {
    const u8* wq = bq ? wqB : wqA;
    const u8* wp = wq + (size_t)(56 * wave + 4 * rrc) * 8192 + (size_t)(col0 + 8 * cc) * 2;
    u32x4 wv[4];
#pragma unroll
    for (int i = 0; i < 4; ++i) wv[i] = ld_nt16(wp + (size_t)i * 8192);
    uint2 xv = *reinterpret_cast<const uint2*>(xin + 2 * (56 * wave + 4 * rrc));
    asm volatile("" ::: "memory");
    __builtin_amdgcn_s_barrier();
    asm volatile("" ::: "memory");
#pragma unroll
    for (int i = 0; i < 4; ++i) KA[i] = ld_nt16(kA + offA + i * 1024);
#pragma unroll
    for (int i = 0; i < 4; ++i) KB[i] = ld_nt16(kB + offB + i * 1024);
    asm volatile("" ::: "memory");
    if (rr >= 14) xv = make_uint2(0u, 0u);
    if (wave == 0 && lane == 0) s_flag[0] = 0u;

    float acc[8];
#pragma unroll
    for (int k = 0; k < 8; ++k) acc[k] = 0.f;
    fma8(acc, bf_lo(xv.x), wv[0]);
    fma8(acc, bf_hi(xv.x), wv[1]);
    fma8(acc, bf_lo(xv.y), wv[2]);
    fma8(acc, bf_hi(xv.y), wv[3]);
    tr_step<4, 8>(acc, lane);
    tr_step<8, 4>(acc, lane);
    acc[0] += swz_xor<16>(acc[0]);
    acc[1] += swz_xor<16>(acc[1]);
    acc[0] += xor32(acc[0]);
    acc[1] += xor32(acc[1]);
    if (lane < 16) *reinterpret_cast<float2*>(&s_o[wave][2 * lane]) = make_float2(acc[0], acc[1]);
  }

  wg_barrier();
  STAMPW(0, 1);

  if (wave == 0) {
    const u64 F = E + 1;
    float s0 = 0.f, s1 = 0.f;
#pragma unroll
    for (int w = 0; w < 8; ++w) {
      const float2 t = *reinterpret_cast<const float2*>(&s_o[w][2 * (lane & 15)]);
      s0 += t.x;
      s1 += t.y;
    }
    {
      const u32 pr = bf_bits(s0) | (bf_bits(s1) << 16);
      const u32 other = swz_xor_u<8>(pr);
      if (lane < 16 && (lane & 8) == 0)
        publish2(ws + WS_QG + (size_t)(bq * 1024 + (col0 >> 2) + 2 * cc + ((lane >> 2) & 1)) * 16, pr, other, F);
    }
    STAMP(3);
    const u8* qa = ws + WS_QG + (size_t)(128 * h + 2 * lane) * 16;
    const u8* qb = qa + (size_t)1024 * 16;
    u32x4 r0 = {0u, 0u, 0u, 0u}, r1 = r0, r2 = r0, r3 = r0;
    bool ok = false;
    for (u32 spin = 0;; ++spin) {
      if (!ok) {
        ld16x4_sc1(qa, qb, r0, r1, r2, r3);
        ok = rec_ok(r0, F) && rec_ok(r1, F) && rec_ok(r2, F) && rec_ok(r3, F);
      }
      if (spin == 0) STAMP(20);
      STAMPV(21, spin + 1);
      if (__all(ok)) break;
      if (spin > kSpinCap) break;
      __builtin_amdgcn_s_sleep(1);
    }
    *reinterpret_cast<u32x4*>(&s_q[0][4 * lane]) = u32x4{r0.x, r0.y, r1.x, r1.y};
    *reinterpret_cast<u32x4*>(&s_q[1][4 * lane]) = u32x4{r2.x, r2.y, r3.x, r3.y};
    if (lane == 0) { s_flag[2] = (u32)E; s_flag[3] = (u32)(E >> 32); }
    asm volatile("s_waitcnt lgkmcnt(0)" ::: "memory");
    if (lane == 0) __hip_atomic_store(&s_flag[0], 1u, __ATOMIC_RELAXED, __HIP_MEMORY_SCOPE_WORKGROUP);
    asm volatile("s_waitcnt lgkmcnt(0)" ::: "memory");
    STAMP(4);
  }

#pragma unroll
  for (int i = 4; i < 16; ++i) KB[i] = ld_nt16(kB + offB + i * 1024);
#pragma unroll
  for (int i = 0; i < 4; ++i) VA[i] = ld_nt16(vA + offA + i * 1024);
#pragma unroll
  for (int i = 0; i < 16 - GM_VLATE; ++i) VB[i] = ld_nt16(vB + offB + i * 1024);
  STAMPW(1, 2);

  {
    u32 spin = 0;
    while (__builtin_amdgcn_readfirstlane(__hip_atomic_load(&s_flag[0], __ATOMIC_RELAXED, __HIP_MEMORY_SCOPE_WORKGROUP)) == 0u) {
      if (++spin > (kSpinCap << 4)) break;
      __builtin_amdgcn_s_sleep(2);
    }
    asm volatile("" ::: "memory");
  }
  STAMPW(1, 5);
  STAMPW(0, 12);
  u64 F;
  {
    const u32 elo = __builtin_amdgcn_readfirstlane(s_flag[2]);
    const u32 ehi = __builtin_amdgcn_readfirstlane(s_flag[3]);
    F = (((u64)ehi << 32) | (u64)elo) + 1;
  }

  const u32x4 qa0 = *reinterpret_cast<const u32x4*>(&s_q[0][4 * c]);
  const u32x4 qa1 = *reinterpret_cast<const u32x4*>(&s_q[0][128 + 4 * c]);
  const u32x4 qb0 = *reinterpret_cast<const u32x4*>(&s_q[1][4 * c]);
  const u32x4 qb1 = *reinterpret_cast<const u32x4*>(&s_q[1][128 + 4 * c]);

  constexpr float kSc = 0.0625f * 1.4426950408889634f;
  float mA, lA;
  {
    float xa[8];
#pragma unroll
    for (int i = 0; i < 4; ++i) {
      xa[2 * i] = dot8(KA[i], qa0);
      xa[2 * i + 1] = dot8(KA[i], qa1);
    }
    tr_step<4, 8>(xa, c);
    tr_step<2, 4>(xa, c);
    tr_step<1, 2>(xa, c);
    float s = xa[0];
    s += swz_xor<8>(s);
    s += swz_xor<16>(s);
    s = bf_round(s) * kSc;
    float m = s;
    m = fmaxf(m, swz_xor<2>(m));
    m = fmaxf(m, swz_xor<4>(m));
    m = fmaxf(m, xor32(m));
    const float p = __builtin_amdgcn_exp2f(s - m);
    float l = p;
    l += swz_xor<2>(l);
    l += swz_xor<4>(l);
    l += xor32(l);
    mA = m;
    lA = l;
    s_p[wave][0][lane] = p;
  }
  float mB, lB;
  {
    float xb[32];
#pragma unroll
    for (int i = 0; i < 16; ++i) {
      xb[2 * i] = dot8(KB[i], qb0);
      xb[2 * i + 1] = dot8(KB[i], qb1);
    }
    tr_step<16, 32>(xb, c);
    tr_step<8, 16>(xb, c);
    tr_step<4, 8>(xb, c);
    tr_step<2, 4>(xb, c);
    tr_step<1, 2>(xb, c);
    const float s = bf_round(xb[0]) * kSc;
    float m = s;
    m = fmaxf(m, swz_xor<2>(m));
    m = fmaxf(m, swz_xor<4>(m));
    m = fmaxf(m, swz_xor<8>(m));
    m = fmaxf(m, swz_xor<16>(m));
    m = fmaxf(m, xor32(m));
    const float p = __builtin_amdgcn_exp2f(s - m);
    float l = p;
    l += swz_xor<2>(l);
    l += swz_xor<4>(l);
    l += swz_xor<8>(l);
    l += swz_xor<16>(l);
    l += xor32(l);
    mB = m;
    lB = l;
    s_p[wave][1][lane] = p;
  }
  if (lane < 2) {
    s_ml[wave][lane][0] = mA;
    s_ml[wave][lane][1] = lA;
    s_ml[wave][2 + lane][0] = mB;
    s_ml[wave][2 + lane][1] = lB;
  }
  asm volatile("s_waitcnt lgkmcnt(0)" ::: "memory");
  STAMPW(1, 6);

#pragma unroll
  for (int i = 16 - GM_VLATE; i < 16; ++i) VB[i] = ld_nt16(vB + offB + i * 1024);
  {
    const u8* wo = ob ? woB : woA;
    const size_t row0 = (size_t)(2 * h + oqh) * 256 + 32 * chunk + 4 * wave;
#pragma unroll
    for (int r = 0; r < 4; ++r) WO[r] = ld_nt16(wo + (row0 + r) * 896 + cl * 16);
  }
  GX = *reinterpret_cast<const u32x4*>(xin + cl * 16);
  GW = *reinterpret_cast<const u32x4*>(gatew + cl * 16);
  asm volatile("" ::: "memory");

  float g_gate, g_omg;
  {
    float gs = 0.f;
    gs += bf_round(bf_lo(GX.x) * bf_lo(GW.x));
    gs += bf_round(bf_hi(GX.x) * bf_hi(GW.x));
    gs += bf_round(bf_lo(GX.y) * bf_lo(GW.y));
    gs += bf_round(bf_hi(GX.y) * bf_hi(GW.y));
    gs += bf_round(bf_lo(GX.z) * bf_lo(GW.z));
    gs += bf_round(bf_hi(GX.z) * bf_hi(GW.z));
    gs += bf_round(bf_lo(GX.w) * bf_lo(GW.w));
    gs += bf_round(bf_hi(GX.w) * bf_hi(GW.w));
    if (lane >= 56) gs = 0.f;
    gs = x32(x16(sum16(gs), false), false);
    gs = bf_round(gs);
    g_gate = bf_round(1.0f / (1.0f + __expf(-gs)));
    g_omg = bf_round(1.0f - g_gate);
  }

  float a0[8], a1[8], b0[8], b1[8];
#pragma unroll
  for (int k = 0; k < 8; ++k) a0[k] = a1[k] = b0[k] = b1[k] = 0.f;
#pragma unroll
  for (int i = 0; i < 4; ++i) {
    const float2 pp = *reinterpret_cast<const float2*>(&s_p[wave][0][hf * 32 + 2 * i]);
    pv_slot(VA[i], pp.x, pp.y, a0, a1);
  }
#pragma unroll
  for (int i = 0; i < 16; ++i) {
    const float2 pp = *reinterpret_cast<const float2*>(&s_p[wave][1][hf * 32 + 2 * i]);
    pv_slot(VB[i], pp.x, pp.y, b0, b1);
  }
  STAMPW(1, 7);
  STAMPW(0, 13);
  STAMP(22 + wave);
  {
    float r0[8], r1[8];
#pragma unroll
    for (int k = 0; k < 8; ++k) {
      const float sa0 = hf ? a0[k] : b0[k], sa1 = hf ? a1[k] : b1[k];
      const float ka0 = hf ? b0[k] : a0[k], ka1 = hf ? b1[k] : a1[k];
      r0[k] = ka0 + px32(sa0, hf);
      r1[k] = ka1 + px32(sa1, hf);
    }
    float4* d0 = reinterpret_cast<float4*>(&s_red[wave][2 * hf][8 * c]);
    float4* d1 = reinterpret_cast<float4*>(&s_red[wave][2 * hf + 1][8 * c]);
    d0[0] = make_float4(r0[0], r0[1], r0[2], r0[3]);
    d0[1] = make_float4(r0[4], r0[5], r0[6], r0[7]);
    d1[0] = make_float4(r1[0], r1[1], r1[2], r1[3]);
    d1[1] = make_float4(r1[4], r1[5], r1[6], r1[7]);
  }
  wg_barrier();
  STAMPW(1, 8);
  {
    const int combo = tid >> 7, dp = tid & 127;
    float M = s_ml[0][combo][0];
#pragma unroll
    for (int w = 1; w < 8; ++w) M = fmaxf(M, s_ml[w][combo][0]);
    float L = 0.f, o0 = 0.f, o1 = 0.f;
#pragma unroll
    for (int w = 0; w < 8; ++w) {
      const float sc = __builtin_amdgcn_exp2f(s_ml[w][combo][0] - M);
      const float2 t = *reinterpret_cast<const float2*>(&s_red[w][combo][2 * dp]);
      L = fmaf(sc, s_ml[w][combo][1], L);
      o0 = fmaf(sc, t.x, o0);
      o1 = fmaf(sc, t.y, o1);
    }
    publish2(ws + WS_PART + ((size_t)(h * 32 + j) * 512 + tid) * 16, __float_as_uint(o0), __float_as_uint(o1), F);
    if (dp == 0) publish2(ws + WS_PML + ((size_t)(h * 32 + j) * 4 + combo) * 16, __float_as_uint(M), __float_as_uint(L), F);
    STAMPW(1, 9);
  }

  {
    const int sl = tid >> 4, r = tid & 15;
    const u8* pv = ws + WS_PART + ((size_t)(h * 32 + sl) * 512 + c4 * 128 + chunk * 16 + r) * 16;
    const u8* pm = ws + WS_PML + ((size_t)(h * 32 + c) * 4 + c4) * 16;
    u32x4 rv = {0u, 0u, 0u, 0u}, rm = rv;
    bool ok = false;
    if (wave == 0) {
      for (u32 spin = 0;; ++spin) {
        if (!ok) {
          ld16x2_sc1(pv, pm, rv, rm);
          ok = rec_ok(rv, F) && rec_ok(rm, F);
        }
        if (__all(ok)) break;
        if (spin > kSpinCap) break;
        __builtin_amdgcn_s_sleep(1);
      }
      const float ms = __uint_as_float(rm.x), ls = __uint_as_float(rm.y);
      const float M = x16(max16(ms), true);
      const float wsc = __builtin_amdgcn_exp2f(ms - M);
      const float L = x16(sum16(wsc * ls), false);
      if (lane < 32) s_w[lane] = wsc * (1.0f / L);
    } else {
      for (u32 spin = 0;; ++spin) {
        if (!ok) {
          rv = ld16_sc1(pv);
          ok = rec_ok(rv, F);
        }
        if (spin == 0) STAMPW(1, 16);
        if (wave == 1) STAMPV(17, spin + 1);
        if (__all(ok)) break;
        if (spin > kSpinCap) break;
        __builtin_amdgcn_s_sleep(1);
      }
    }
    STAMPW(1, 10);
    wg_barrier();
    const float wmine = s_w[sl];
    float o0 = wmine * __uint_as_float(rv.x), o1 = wmine * __uint_as_float(rv.y);
    o0 = x32(x16(o0, false), false);
    o1 = x32(x16(o1, false), false);
    if (lane < 16) *reinterpret_cast<float2*>(&s_o[wave][2 * lane]) = make_float2(o0, o1);
    wg_barrier();
    float od[4] = {0.f, 0.f, 0.f, 0.f};
#pragma unroll
    for (int w = 0; w < 8; ++w) {
      const float4 t = *reinterpret_cast<const float4*>(&s_o[w][4 * wave]);
      od[0] += t.x;
      od[1] += t.y;
      od[2] += t.z;
      od[3] += t.w;
    }
    float y[8];
#pragma unroll
    for (int k = 0; k < 8; ++k) y[k] = 0.f;
#pragma unroll
    for (int q = 0; q < 4; ++q) fma8(y, bf_round(od[q]), WO[q]);
    float* yred = &s_red[0][0][0];
    if (lane < 56) {
      float4* d = reinterpret_cast<float4*>(yred + wave * 448 + 8 * lane);
      d[0] = make_float4(y[0], y[1], y[2], y[3]);
      d[1] = make_float4(y[4], y[5], y[6], y[7]);
    }
    wg_barrier();
    if (tid < 224) {
      float s0 = 0.f, s1 = 0.f;
#pragma unroll
      for (int w = 0; w < 8; ++w) {
        const float2 t = *reinterpret_cast<const float2*>(yred + w * 448 + 2 * tid);
        s0 += t.x;
        s1 += t.y;
      }
      publish2(ws + WS_Y + ((size_t)wg * 224 + tid) * 16, __float_as_uint(s0), __float_as_uint(s1), F);
      STAMPW(1, 11);
    }
  }

  if (wg < kNRed) {
    const int p = tid >> 1, hh = tid & 1;
    const u8* yp = ws + WS_Y + ((size_t)p * 224 + 4 * wg + 2 * hh) * 16;
    u32x4 r0 = {0u, 0u, 0u, 0u}, r1 = r0;
    bool ok = false;
    for (u32 spin = 0;; ++spin) {
      if (!ok) {
        ld16x2_sc1(yp, yp + 16, r0, r1);
        ok = rec_ok(r0, F) && rec_ok(r1, F);
      }
      if (spin == 0) STAMPW(0, 18);
      if (wave == 0) STAMPV(19, spin + 1);
      if (__all(ok)) break;
      if (spin > kSpinCap) break;
      __builtin_amdgcn_s_sleep(1);
    }
    STAMPW(0, 14);
    float v0 = __uint_as_float(r0.x), v1 = __uint_as_float(r0.y), v2 = __uint_as_float(r1.x), v3 = __uint_as_float(r1.y);
    v0 += dpp_ror<8>(v0); v1 += dpp_ror<8>(v1); v2 += dpp_ror<8>(v2); v3 += dpp_ror<8>(v3);
    v0 += dpp_ror<4>(v0); v1 += dpp_ror<4>(v1); v2 += dpp_ror<4>(v2); v3 += dpp_ror<4>(v3);
    v0 += dpp_ror<2>(v0); v1 += dpp_ror<2>(v1); v2 += dpp_ror<2>(v2); v3 += dpp_ror<2>(v3);
    v0 = x32(x16(v0, false), false); v1 = x32(x16(v1, false), false);
    v2 = x32(x16(v2, false), false); v3 = x32(x16(v3, false), false);
    if (lane < 2) *reinterpret_cast<float4*>(&s_o[wave][4 * lane]) = make_float4(v0, v1, v2, v3);
    wg_barrier();
    if (wave == 0) {
      if (lane < 4) {
        float ra[2], rb[2];
#pragma unroll
        for (int e = 0; e < 2; ++e) {
          const int col = 2 * lane + e;
          ra[e] = (s_o[0][col] + s_o[1][col]) + (s_o[2][col] + s_o[3][col]);
          rb[e] = (s_o[4][col] + s_o[5][col]) + (s_o[6][col] + s_o[7][col]);
        }
        const float r0f = bf_round(g_gate * bf_round(ra[0])) + bf_round(g_omg * bf_round(rb[0]));
        const float r1f = bf_round(g_gate * bf_round(ra[1])) + bf_round(g_omg * bf_round(rb[1]));
        *reinterpret_cast<u32*>(out + 8 * wg + 2 * lane) = bf_bits(r0f) | (bf_bits(r1f) << 16);
      }
      if (wg == 0 && lane == 0) st_agent(ws + WS_EPOCH, F);
      STAMP(15);
    }
  }
}

}

extern "C" void kernel_launch(void* const* d_in, const int* in_sizes, int n_in, void* d_out, int out_size,
                              void* d_ws, size_t ws_size, hipStream_t stream) {
  (void)in_sizes; (void)n_in; (void)out_size; (void)ws_size;
  const u8* x = (const u8*)d_in[0];
  const u8* kA = (const u8*)d_in[1];
  const u8* vA = (const u8*)d_in[2];
  const u8* wqA = (const u8*)d_in[3];
  const u8* woA = (const u8*)d_in[4];
  const u8* kB = (const u8*)d_in[6];
  const u8* vB = (const u8*)d_in[7];
  const u8* wqB = (const u8*)d_in[8];
  const u8* woB = (const u8*)d_in[9];
  const u8* gw = (const u8*)d_in[10];
  gm_kernel<<<kNWG, kThreads, 0, stream>>>(x, kA, vA, wqA, woA, kB, vB, wqB, woB, gw, (unsigned short*)d_out, (u8*)d_ws);
}
```
